```python
import torch

def _softmax(x, axis=-1):
    return torch.softmax(x, dim=axis)
def _gelu(x):
    return torch.nn.functional.gelu(x, approximate="tanh")
def _silu(x):
    return torch.nn.functional.silu(x)
def _sigmoid(x):
    return torch.sigmoid(x)
def _layernorm(x, axis=-1, eps=1e-5):
    m = x.mean(dim=axis, keepdim=True)
    v = x.var(dim=axis, keepdim=True, unbiased=False)
    return (x - m) / torch.sqrt(v + eps)
def _scatter_add(tgt, idx, src, axis=0):
    return tgt.clone().index_add_(axis, idx, src)
def _segment_sum(src, seg, n):
    out = torch.zeros((n,) + src.shape[1:], dtype=src.dtype)
    return out.index_add_(0, seg, src)
def _pad(x, pw):
    flat = tuple(v for pair in reversed(pw) for v in pair)
    return torch.nn.functional.pad(x, flat)

kernel_name = 'standalone__mha_block_cached__near_ridge'

def setup_inputs():

    _w = (lambda a: (lambda t: t.to(torch.bfloat16) if t.is_floating_point() else t)(torch.as_tensor(a)))
    B, Sq, D = 16, 128, 512
    H, Sk, Dh = 32, 512, 128
    return {
        "x": _w((lambda *s: torch.randn(*s, dtype=torch.bfloat16) * 0.8)(B, Sq, D)),
        "k_cache": _w((lambda *s: torch.randn(*s, dtype=torch.bfloat16) * 0.8)(B, H, Sk, Dh)),
        "v_cache": _w((lambda *s: torch.randn(*s, dtype=torch.bfloat16) * 0.05)(B, H, Sk, Dh)),
        "wq": _w((lambda *s: torch.randn(*s, dtype=torch.bfloat16) * 0.8)(D, H * Dh) * (D ** -0.5)),
        "wo": _w((lambda *s: torch.randn(*s, dtype=torch.bfloat16) * 0.05)(H * Dh, D) * ((H * Dh) ** -0.5)),
    }

def reference(x, k_cache, v_cache, wq, wo):
    B, Sq, D = x.shape
    H, Dh = 32, 128
    q = (lambda x, *d: x.permute(*d))((x @ wq).reshape(B, Sq, H, Dh), 0, 2, 1, 3)
    scores = torch.einsum("bhqd,bhkd->bhqk", q, k_cache) * (Dh ** -0.5)
    p = _softmax(scores, axis=-1)
    o = torch.einsum("bhqk,bhkd->bhqd", p, v_cache)
    return (lambda x, *d: x.permute(*d))(o, 0, 2, 1, 3).reshape(B, Sq, H * Dh) @ wo
```

```cpp
#include <hip/hip_runtime.h>
#include <hip/hip_bf16.h>
#include <stdint.h>

typedef __bf16 bf16x8 __attribute__((ext_vector_type(8)));
typedef __bf16 bf16x4 __attribute__((ext_vector_type(4)));
typedef __bf16 bf16x2 __attribute__((ext_vector_type(2)));
typedef float f32x16 __attribute__((ext_vector_type(16)));
typedef float f32x4 __attribute__((ext_vector_type(4)));
typedef float f32x2 __attribute__((ext_vector_type(2)));
typedef unsigned u32x4 __attribute__((ext_vector_type(4)));
#define LAS __attribute__((address_space(3)))
#define GAS __attribute__((address_space(1)))
#define DI __device__ __forceinline__

constexpr int NB = 16, SQ = 128, DM = 512, NH = 32, SK = 512, DH = 128;
constexpr int HD = NH * DH;

DI unsigned rfl(unsigned x) { return __builtin_amdgcn_readfirstlane(x); }
DI unsigned cvtpk(float lo, float hi) {
  f32x2 v = {lo, hi};
  bf16x2 b = __builtin_convertvector(v, bf16x2);
  return __builtin_bit_cast(unsigned, b);
}
DI bf16x8 pack8(const f32x16& x, int s) {
  u32x4 w = {cvtpk(x[8 * s + 0], x[8 * s + 1]), cvtpk(x[8 * s + 2], x[8 * s + 3]), cvtpk(x[8 * s + 4], x[8 * s + 5]),
             cvtpk(x[8 * s + 6], x[8 * s + 7])};
  return __builtin_bit_cast(bf16x8, w);
}
#define MFMA(a, b, c) __builtin_amdgcn_mfma_f32_32x32x16_bf16((a), (b), (c), 0, 0, 0)

DI void dma16(const void* sbase, unsigned voff, unsigned ldsdst) {
  asm volatile("s_mov_b32 m0, %2\n\ts_nop 0\n\tglobal_load_lds_dwordx4 %0, %1" ::"v"(voff), "s"(sbase), "s"(ldsdst) : "memory");
}
template <int N> DI void wait_vm() { asm volatile("s_waitcnt vmcnt(%0)" ::"i"(N) : "memory"); }
DI void bar() {
  __builtin_amdgcn_sched_barrier(0);
  asm volatile("s_waitcnt lgkmcnt(0)" ::: "memory");
  __builtin_amdgcn_s_barrier();
  asm volatile("" ::: "memory");
  __builtin_amdgcn_sched_barrier(0);
}
DI bf16x8 lds_b128(unsigned addr) { return *(const LAS bf16x8*)(uintptr_t)addr; }
DI bf16x4 lds_tr(unsigned addr) { return __builtin_amdgcn_ds_read_tr16_b64_v4bf16((LAS bf16x4*)(uintptr_t)addr); }
DI bf16x8 cat(bf16x4 a, bf16x4 b) { return __builtin_shufflevector(a, b, 0, 1, 2, 3, 4, 5, 6, 7); }

constexpr int SLOT = 16384, NSLOT = 5;

__global__ __launch_bounds__(256, 2) void attn_kernel(const __hip_bfloat16* __restrict__ x, const __hip_bfloat16* __restrict__ kc,
                                                      const __hip_bfloat16* __restrict__ vc, const __hip_bfloat16* __restrict__ wq,
                                                      u32x4* __restrict__ ofrag) {
  const unsigned tid = threadIdx.x, lane = tid & 63u, wid = rfl(tid >> 6), r = lane & 31u, hi = lane >> 5;
  const unsigned bid = blockIdx.x, xcd = bid & 7u, idx = bid >> 3;
  const unsigned b = (xcd & 1u) * 8u + (idx & 7u), h = (xcd >> 1) * 8u + (idx >> 3);
  constexpr unsigned ldsb = 0;
  const char* xb = (const char*)x + (size_t)b * SQ * DM * 2;
  const char* wqb = (const char*)wq + (size_t)h * DH * 2;
  const char* kb = (const char*)kc + ((size_t)b * NH + h) * SK * DH * 2;
  const char* vb = (const char*)vc + ((size_t)b * NH + h) * SK * DH * 2;

  const unsigned xr = lane >> 3, xc = lane & 7u;
  unsigned xsrc[2];
  xsrc[0] = (wid * 32u + xr) * (DM * 2) + 16u * (xc ^ ((xr >> 1) & 7u));
  xsrc[1] = (wid * 32u + 8u + xr) * (DM * 2) + 16u * (xc ^ ((4u + (xr >> 1)) & 7u));
  const unsigned r4 = lane >> 4, c16 = lane & 15u;
  const unsigned wsrc = (wid * 16u + r4) * (HD * 2) + 16u * (c16 ^ (r4 << 2));
  const unsigned vsrc = (wid * 16u + r4) * 256u + 16u * (c16 ^ (r4 << 2));
  unsigned ksrc[4];
#pragma unroll
  for (int q = 0; q < 4; ++q) ksrc[q] = (wid * 16u + 4u * q + r4) * 256u + 16u * (c16 ^ (4u * q + r4));

  auto issue = [&](int L) {
    const unsigned dst = ldsb + (unsigned)(L % NSLOT) * SLOT + wid * 4096u;
    if (L < 16) {
      const int c = L >> 1;
      if ((L & 1) == 0) {
#pragma unroll
        for (int q = 0; q < 4; ++q) dma16(xb + c * 128 + (q >> 1) * 16 * DM * 2, xsrc[q & 1], dst + q * 1024u);
      } else {
#pragma unroll
        for (int q = 0; q < 4; ++q) dma16(wqb + (size_t)(c * 64 + q * 4) * HD * 2, wsrc, dst + q * 1024u);
      }
    } else {
      const int t = (L - 16) >> 1;
      if ((L & 1) == 0) {
#pragma unroll
        for (int q = 0; q < 4; ++q) dma16(kb + t * 16384, ksrc[q], dst + q * 1024u);
      } else {
#pragma unroll
        for (int q = 0; q < 4; ++q) dma16(vb + t * 16384 + q * 1024, vsrc, dst + q * 1024u);
      }
    }
  };

  const unsigned g1 = (lane >> 4) & 1u, q4 = (lane >> 2) & 3u, p4 = lane & 3u;
  const unsigned sp = ((p4 & 1u) << 1) | (p4 >> 1);
  unsigned trb[4];
#pragma unroll
  for (int c = 0; c < 4; ++c) trb[c] = (8u * hi + q4) * 256u + 64u * ((unsigned)c ^ q4) + 32u * g1 + 8u * sp;
  unsigned vtb[4];
#pragma unroll
  for (int c = 0; c < 4; ++c) vtb[c] = (4u * hi + q4) * 256u + 64u * ((unsigned)c ^ q4) + 32u * g1 + 8u * sp;
  const unsigned xrow = wid * 32u + r;
  const unsigned xrd = xrow * 128u + 16u * (hi ^ ((xrow >> 1) & 7u));
  const unsigned krd = r * 256u + 16u * (hi ^ (r & 15u));

#pragma unroll
  for (int L = 0; L < NSLOT; ++L) issue(L);

  f32x16 acc[4];
#pragma unroll
  for (int i = 0; i < 4; ++i) acc[i] = f32x16{};
#pragma unroll
  for (int c = 0; c < 8; ++c) {
    if (c == 0) wait_vm<12>(); else wait_vm<4>();
    bar();
    if (c > 0) { issue(2 * c + 3); issue(2 * c + 4); }
    const unsigned xs = ldsb + (unsigned)((2 * c) % NSLOT) * SLOT, ws = ldsb + (unsigned)((2 * c + 1) % NSLOT) * SLOT;
#pragma unroll
    for (int s = 0; s < 4; ++s) {
      const bf16x8 bx = lds_b128(xs + (xrd ^ (32u * s)));
#pragma unroll
      for (int i = 0; i < 4; ++i) {
        const bf16x8 a = cat(lds_tr(ws + trb[i] + 4096u * s), lds_tr(ws + trb[i] + 4096u * s + 1024u));
        acc[i] = MFMA(a, bx, acc[i]);
      }
    }
  }
  bf16x8 qf[8];
#pragma unroll
  for (int i = 0; i < 4; ++i) { qf[2 * i] = pack8(acc[i], 0); qf[2 * i + 1] = pack8(acc[i], 1); }

  constexpr float C = 0.08838834764831845f * 1.4426950408889634f;
  float m = -1e30f, l = 0.f;
  f32x16 o[4];
#pragma unroll
  for (int i = 0; i < 4; ++i) o[i] = f32x16{};
#pragma unroll
  for (int t = 0; t < 8; ++t) {
    const int LK = 16 + 2 * t, LV = LK + 1;
    {
      const int issued = (t == 0) ? 19 : (LK + 4 > 32 ? 32 : LK + 4);
      const int younger = issued - LK - 1;
      if (younger == 3) wait_vm<12>(); else if (younger == 2) wait_vm<8>(); else if (younger == 1) wait_vm<4>(); else wait_vm<0>();
    }
    bar();
    if (t == 0) { issue(19); issue(20); } else if (LK + 4 < 32) issue(LK + 4);
    const unsigned ks = ldsb + (unsigned)(LK % NSLOT) * SLOT;
    f32x16 s0 = f32x16{}, s1 = f32x16{};
#pragma unroll
    for (int s = 0; s < 8; ++s) {
      const bf16x8 a0 = lds_b128(ks + (krd ^ (32u * s)));
      const bf16x8 a1 = lds_b128(ks + 8192u + (krd ^ (32u * s)));
      s0 = MFMA(a0, qf[s], s0);
      s1 = MFMA(a1, qf[s], s1);
    }
    float mx = s0[0];
#pragma unroll
    for (int i = 1; i < 16; ++i) mx = fmaxf(mx, s0[i]);
#pragma unroll
    for (int i = 0; i < 16; ++i) mx = fmaxf(mx, s1[i]);
    {
      auto rr = __builtin_amdgcn_permlane32_swap(__float_as_uint(mx), __float_as_uint(mx), false, false);
      mx = fmaxf(__uint_as_float(rr[0]), __uint_as_float(rr[1]));
    }
    const float mn = fmaxf(m, mx);
    const float alpha = __builtin_amdgcn_exp2f((m - mn) * C);
    m = mn;
    const float nm = -mn * C;
    float ps = 0.f;
#pragma unroll
    for (int i = 0; i < 16; ++i) { s0[i] = __builtin_amdgcn_exp2f(fmaf(s0[i], C, nm)); ps += s0[i]; }
#pragma unroll
    for (int i = 0; i < 16; ++i) { s1[i] = __builtin_amdgcn_exp2f(fmaf(s1[i], C, nm)); ps += s1[i]; }
    l = l * alpha + ps;
    asm volatile("" : "+v"(l));
#pragma unroll
    for (int i = 0; i < 4; ++i) o[i] *= alpha;
    bf16x8 pf[4] = {pack8(s0, 0), pack8(s0, 1), pack8(s1, 0), pack8(s1, 1)};
    {
      const int issued = (t == 0) ? 21 : (LV + 4 > 32 ? 32 : LV + 4);
      const int younger = issued - LV - 1;
      if (younger == 3) wait_vm<12>(); else if (younger == 2) wait_vm<8>(); else if (younger == 1) wait_vm<4>(); else wait_vm<0>();
    }
    bar();
    if (LV + 4 < 32) issue(LV + 4);
    const unsigned vs = ldsb + (unsigned)(LV % NSLOT) * SLOT;
#pragma unroll
    for (int s = 0; s < 4; ++s) {
#pragma unroll
      for (int i = 0; i < 4; ++i) {
        const bf16x8 a = cat(lds_tr(vs + vtb[i] + 4096u * s), lds_tr(vs + vtb[i] + 4096u * s + 2048u));
        o[i] = MFMA(a, pf[s], o[i]);
      }
    }
  }
  {
    auto rr = __builtin_amdgcn_permlane32_swap(__float_as_uint(l), __float_as_uint(l), false, false);
    l = __uint_as_float(rr[0]) + __uint_as_float(rr[1]);
  }
  const float il = 1.0f / l;
  u32x4* dst = ofrag + (((size_t)b * 4 + wid) * NH + h) * 8 * 64 + lane;
#pragma unroll
  for (int i = 0; i < 4; ++i) {
    o[i] *= il;
    dst[(2 * i) * 64] = __builtin_bit_cast(u32x4, pack8(o[i], 0));
    dst[(2 * i + 1) * 64] = __builtin_bit_cast(u32x4, pack8(o[i], 1));
  }
}

constexpr int PF = 8;
__global__ __launch_bounds__(512, 2) void oproj_kernel(const u32x4* __restrict__ ofrag, const __hip_bfloat16* __restrict__ wo,
                                                       __hip_bfloat16* __restrict__ out) {
  extern __shared__ __attribute__((aligned(16))) char lds[];
  const unsigned tid = threadIdx.x, lane = tid & 63u, wid = rfl(tid >> 6), hi = lane >> 5;
  const unsigned bid = blockIdx.x, xcd = bid & 7u, idx = bid >> 3;
  const unsigned mb = xcd * 4u + (idx & 3u), nb = idx >> 2;
  const unsigned ldsb = wid * (PF * 2048u);
  const unsigned wr = lane >> 3, wc = lane & 7u;
  const unsigned wsrc = wr * (DM * 2) + 16u * (wc ^ (((wr >> 1) & 1u) << 2));
  const char* wb = (const char*)wo + (size_t)wid * 512 * DM * 2 + nb * 128u;
  const u32x4* of0 = ofrag + ((size_t)(mb * 2) * NH + wid * 4) * 8 * 64 + lane;
  const unsigned g1 = (lane >> 4) & 1u, q4 = (lane >> 2) & 3u, p4 = lane & 3u;
  const unsigned sp = ((p4 & 1u) << 1) | (p4 >> 1);
  unsigned trb[2];
#pragma unroll
  for (int c = 0; c < 2; ++c) trb[c] = ldsb + (8u * hi + q4) * 128u + 64u * ((unsigned)c ^ (q4 >> 1)) + 32u * g1 + 8u * sp;

  u32x4 bq[PF][2];
  auto issue = [&](int ks) {
    const unsigned dst = ldsb + (unsigned)(ks % PF) * 2048u;
    dma16(wb + (size_t)(ks * 16) * DM * 2, wsrc, dst);
    dma16(wb + (size_t)(ks * 16 + 8) * DM * 2, wsrc, dst + 1024u);
    asm volatile("global_load_dwordx4 %0, %1, off" : "=v"(bq[ks % PF][0]) : "v"(of0 + ks * 64) : "memory");
    asm volatile("global_load_dwordx4 %0, %1, off" : "=v"(bq[ks % PF][1]) : "v"(of0 + NH * 8 * 64 + ks * 64) : "memory");
  };
  f32x16 acc[2][2];
#pragma unroll
  for (int i = 0; i < 2; ++i)
#pragma unroll
    for (int j = 0; j < 2; ++j) acc[i][j] = f32x16{};
#pragma unroll
  for (int ks = 0; ks < PF; ++ks) issue(ks);
#pragma unroll
  for (int ks = 0; ks < 32; ++ks) {
    const int younger = (ks + PF <= 32 ? PF : 32 - ks) - 1;
    switch (younger) {
      case 7: asm volatile("s_waitcnt vmcnt(28)" : "+v"(bq[ks % PF][0]), "+v"(bq[ks % PF][1])::"memory"); break;
      case 6: asm volatile("s_waitcnt vmcnt(24)" : "+v"(bq[ks % PF][0]), "+v"(bq[ks % PF][1])::"memory"); break;
      case 5: asm volatile("s_waitcnt vmcnt(20)" : "+v"(bq[ks % PF][0]), "+v"(bq[ks % PF][1])::"memory"); break;
      case 4: asm volatile("s_waitcnt vmcnt(16)" : "+v"(bq[ks % PF][0]), "+v"(bq[ks % PF][1])::"memory"); break;
      case 3: asm volatile("s_waitcnt vmcnt(12)" : "+v"(bq[ks % PF][0]), "+v"(bq[ks % PF][1])::"memory"); break;
      case 2: asm volatile("s_waitcnt vmcnt(8)" : "+v"(bq[ks % PF][0]), "+v"(bq[ks % PF][1])::"memory"); break;
      case 1: asm volatile("s_waitcnt vmcnt(4)" : "+v"(bq[ks % PF][0]), "+v"(bq[ks % PF][1])::"memory"); break;
      default: asm volatile("s_waitcnt vmcnt(0)" : "+v"(bq[ks % PF][0]), "+v"(bq[ks % PF][1])::"memory"); break;
    }
    const unsigned so = (unsigned)(ks % PF) * 2048u;
    bf16x8 a[2];
#pragma unroll
    for (int c = 0; c < 2; ++c) a[c] = cat(lds_tr(trb[c] + so), lds_tr(trb[c] + so + 512u));
    const bf16x8 b0 = __builtin_bit_cast(bf16x8, bq[ks % PF][0]), b1 = __builtin_bit_cast(bf16x8, bq[ks % PF][1]);
#pragma unroll
    for (int c = 0; c < 2; ++c) {
      acc[c][0] = MFMA(a[c], b0, acc[c][0]);
      acc[c][1] = MFMA(a[c], b1, acc[c][1]);
    }
    if (ks + PF < 32) {
      asm volatile("s_waitcnt lgkmcnt(0)" ::: "memory");
      __builtin_amdgcn_sched_barrier(0);
      issue(ks + PF);
    }
  }
  __syncthreads();
  f32x4* red = (f32x4*)lds;
#pragma unroll
  for (int c = 0; c < 2; ++c)
#pragma unroll
    for (int j = 0; j < 2; ++j)
#pragma unroll
      for (int g = 0; g < 4; ++g) {
        f32x4 v = {acc[c][j][4 * g], acc[c][j][4 * g + 1], acc[c][j][4 * g + 2], acc[c][j][4 * g + 3]};
        red[((wid * 4 + c * 2 + j) * 4 + g) * 64 + lane] = v;
      }
  __syncthreads();
  const unsigned tile = wid >> 1, oct = wid & 1u;
  f32x4 s0 = {0, 0, 0, 0}, s1 = {0, 0, 0, 0};
#pragma unroll
  for (int w = 0; w < 8; ++w) {
    s0 += red[((w * 4 + tile) * 4 + 2 * oct) * 64 + lane];
    s1 += red[((w * 4 + tile) * 4 + 2 * oct + 1) * 64 + lane];
  }
  u32x4 w4 = {cvtpk(s0[0], s0[1]), cvtpk(s0[2], s0[3]), cvtpk(s1[0], s1[1]), cvtpk(s1[2], s1[3])};
  const unsigned c = tile >> 1, j = tile & 1u;
  const unsigned row = mb * 64u + j * 32u + (lane & 31u), col = nb * 64u + c * 32u + oct * 16u + hi * 8u;
  *(u32x4*)((char*)out + ((size_t)row * DM + col) * 2) = w4;
}

extern "C" void kernel_launch(void* const* d_in, const int* in_sizes, int n_in, void* d_out, int out_size, void* d_ws, size_t ws_size,
                              hipStream_t stream) {
  const __hip_bfloat16* x = (const __hip_bfloat16*)d_in[0];
  const __hip_bfloat16* kc = (const __hip_bfloat16*)d_in[1];
  const __hip_bfloat16* vc = (const __hip_bfloat16*)d_in[2];
  const __hip_bfloat16* wq = (const __hip_bfloat16*)d_in[3];
  const __hip_bfloat16* wo = (const __hip_bfloat16*)d_in[4];
  u32x4* ofrag = (u32x4*)d_ws;
  (void)hipFuncSetAttribute((const void*)attn_kernel, hipFuncAttributeMaxDynamicSharedMemorySize, NSLOT * SLOT);
  (void)hipFuncSetAttribute((const void*)oproj_kernel, hipFuncAttributeMaxDynamicSharedMemorySize, 131072);
  attn_kernel<<<512, 256, NSLOT * SLOT, stream>>>(x, kc, vc, wq, ofrag);
  oproj_kernel<<<256, 512, 131072, stream>>>(ofrag, wo, (__hip_bfloat16*)d_out);
}
```

```cpp
#include <hip/hip_runtime.h>
#include <hip/hip_bf16.h>
#include <stdint.h>

typedef __bf16 bf16x8 __attribute__((ext_vector_type(8)));
typedef __bf16 bf16x4 __attribute__((ext_vector_type(4)));
typedef __bf16 bf16x2 __attribute__((ext_vector_type(2)));
typedef float f32x16 __attribute__((ext_vector_type(16)));
typedef float f32x4 __attribute__((ext_vector_type(4)));
typedef float f32x2 __attribute__((ext_vector_type(2)));
typedef unsigned u32x4 __attribute__((ext_vector_type(4)));
#define LAS __attribute__((address_space(3)))
#define GAS __attribute__((address_space(1)))
#define DI __device__ __forceinline__

constexpr int NB = 16, SQ = 128, DM = 512, NH = 32, SK = 512, DH = 128;
constexpr int HD = NH * DH;

DI unsigned rfl(unsigned x) { return __builtin_amdgcn_readfirstlane(x); }
DI unsigned cvtpk(float lo, float hi) {
  f32x2 v = {lo, hi};
  bf16x2 b = __builtin_convertvector(v, bf16x2);
  return __builtin_bit_cast(unsigned, b);
}
DI bf16x8 pack8(const f32x16& x, int s) {
  u32x4 w = {cvtpk(x[8 * s + 0], x[8 * s + 1]), cvtpk(x[8 * s + 2], x[8 * s + 3]), cvtpk(x[8 * s + 4], x[8 * s + 5]),
             cvtpk(x[8 * s + 6], x[8 * s + 7])};
  return __builtin_bit_cast(bf16x8, w);
}
#define MFMA(a, b, c) __builtin_amdgcn_mfma_f32_32x32x16_bf16((a), (b), (c), 0, 0, 0)

DI void dma16(const void* sbase, unsigned voff, unsigned ldsdst) {
  asm volatile("s_mov_b32 m0, %2\n\ts_nop 0\n\tglobal_load_lds_dwordx4 %0, %1" ::"v"(voff), "s"(sbase), "s"(ldsdst) : "memory");
}
DI void dma16nt(const void* sbase, unsigned voff, unsigned ldsdst) {
  asm volatile("s_mov_b32 m0, %2\n\ts_nop 0\n\tglobal_load_lds_dwordx4 %0, %1 nt" ::"v"(voff), "s"(sbase), "s"(ldsdst) : "memory");
}
template <int N> DI void wait_vm() { asm volatile("s_waitcnt vmcnt(%0)" ::"i"(N) : "memory"); }
DI void bar() {
  __builtin_amdgcn_sched_barrier(0);
  asm volatile("s_waitcnt lgkmcnt(0)" ::: "memory");
  __builtin_amdgcn_s_barrier();
  asm volatile("" ::: "memory");
  __builtin_amdgcn_sched_barrier(0);
}
DI bf16x8 lds_b128(unsigned addr) { return *(const LAS bf16x8*)(uintptr_t)addr; }
DI bf16x4 lds_tr(unsigned addr) { return __builtin_amdgcn_ds_read_tr16_b64_v4bf16((LAS bf16x4*)(uintptr_t)addr); }
DI bf16x8 cat(bf16x4 a, bf16x4 b) { return __builtin_shufflevector(a, b, 0, 1, 2, 3, 4, 5, 6, 7); }

constexpr int SLOT = 16384, NSLOT = 5;

__global__ __launch_bounds__(256, 2) void attn_kernel(const __hip_bfloat16* __restrict__ x, const __hip_bfloat16* __restrict__ kc,
                                                      const __hip_bfloat16* __restrict__ vc, const __hip_bfloat16* __restrict__ wq,
                                                      u32x4* __restrict__ ofrag) {
  const unsigned tid = threadIdx.x, lane = tid & 63u, wid = rfl(tid >> 6), r = lane & 31u, hi = lane >> 5;
  const unsigned bid = blockIdx.x, xcd = bid & 7u, idx = bid >> 3;
  const unsigned b = (xcd & 1u) * 8u + (idx & 7u), h = (xcd >> 1) * 8u + (idx >> 3);
  constexpr unsigned ldsb = 0;
  const char* xb = (const char*)x + (size_t)b * SQ * DM * 2;
  const char* wqb = (const char*)wq + (size_t)h * DH * 2;
  const char* kb = (const char*)kc + ((size_t)b * NH + h) * SK * DH * 2;
  const char* vb = (const char*)vc + ((size_t)b * NH + h) * SK * DH * 2;

  const unsigned xr = lane >> 3, xc = lane & 7u;
  unsigned xsrc[2];
  xsrc[0] = (wid * 32u + xr) * (DM * 2) + 16u * (xc ^ ((xr >> 1) & 7u));
  xsrc[1] = (wid * 32u + 8u + xr) * (DM * 2) + 16u * (xc ^ ((4u + (xr >> 1)) & 7u));
  const unsigned r4 = lane >> 4, c16 = lane & 15u;
  const unsigned wsrc = (wid * 16u + r4) * (HD * 2) + 16u * (c16 ^ (r4 << 2));
  const unsigned vsrc = (wid * 16u + r4) * 256u + 16u * (c16 ^ (r4 << 2));
  unsigned ksrc[4];
#pragma unroll
  for (int q = 0; q < 4; ++q) ksrc[q] = (wid * 16u + 4u * q + r4) * 256u + 16u * (c16 ^ (4u * q + r4));

  auto issue = [&](int L) {
    const unsigned dst = ldsb + (unsigned)(L % NSLOT) * SLOT + wid * 4096u;
    if (L < 16) {
      const int c = L >> 1;
      if ((L & 1) == 0) {
#pragma unroll
        for (int q = 0; q < 4; ++q) dma16(xb + c * 128 + (q >> 1) * 16 * DM * 2, xsrc[q & 1], dst + q * 1024u);
      } else {
#pragma unroll
        for (int q = 0; q < 4; ++q) dma16(wqb + (size_t)(c * 64 + q * 4) * HD * 2, wsrc, dst + q * 1024u);
      }
    } else {
      const int t = (L - 16) >> 1;
      if ((L & 1) == 0) {
#pragma unroll
        for (int q = 0; q < 4; ++q) dma16nt(kb + t * 16384, ksrc[q], dst + q * 1024u);
      } else {
#pragma unroll
        for (int q = 0; q < 4; ++q) dma16nt(vb + t * 16384 + q * 1024, vsrc, dst + q * 1024u);
      }
    }
  };

  const unsigned g1 = (lane >> 4) & 1u, q4 = (lane >> 2) & 3u, p4 = lane & 3u;
  const unsigned sp = ((p4 & 1u) << 1) | (p4 >> 1);
  unsigned trb[4];
#pragma unroll
  for (int c = 0; c < 4; ++c) trb[c] = (8u * hi + q4) * 256u + 64u * ((unsigned)c ^ q4) + 32u * g1 + 8u * sp;
  unsigned vtb[4];
#pragma unroll
  for (int c = 0; c < 4; ++c) vtb[c] = (4u * hi + q4) * 256u + 64u * ((unsigned)c ^ q4) + 32u * g1 + 8u * sp;
  const unsigned xrow = wid * 32u + r;
  const unsigned xrd = xrow * 128u + 16u * (hi ^ ((xrow >> 1) & 7u));
  const unsigned krd = r * 256u + 16u * (hi ^ (r & 15u));

#pragma unroll
  for (int L = 0; L < NSLOT; ++L) issue(L);

  f32x16 acc[4];
#pragma unroll
  for (int i = 0; i < 4; ++i) acc[i] = f32x16{};
#pragma unroll
  for (int c = 0; c < 8; ++c) {
    if (c == 0) wait_vm<12>(); else wait_vm<4>();
    bar();
    if (c > 0) { issue(2 * c + 3); issue(2 * c + 4); }
    const unsigned xs = ldsb + (unsigned)((2 * c) % NSLOT) * SLOT, ws = ldsb + (unsigned)((2 * c + 1) % NSLOT) * SLOT;
#pragma unroll
    for (int s = 0; s < 4; ++s) {
      const bf16x8 bx = lds_b128(xs + (xrd ^ (32u * s)));
#pragma unroll
      for (int i = 0; i < 4; ++i) {
        const bf16x8 a = cat(lds_tr(ws + trb[i] + 4096u * s), lds_tr(ws + trb[i] + 4096u * s + 1024u));
        acc[i] = MFMA(a, bx, acc[i]);
      }
    }
  }
  bf16x8 qf[8];
#pragma unroll
  for (int i = 0; i < 4; ++i) { qf[2 * i] = pack8(acc[i], 0); qf[2 * i + 1] = pack8(acc[i], 1); }

  constexpr float C = 0.08838834764831845f * 1.4426950408889634f;
  float m = -1e30f, l = 0.f;
  f32x16 o[4];
#pragma unroll
  for (int i = 0; i < 4; ++i) o[i] = f32x16{};
#pragma unroll
  for (int t = 0; t < 8; ++t) {
    const int LK = 16 + 2 * t, LV = LK + 1;
    {
      const int issued = (t == 0) ? 19 : (LK + 4 > 32 ? 32 : LK + 4);
      const int younger = issued - LK - 1;
      if (younger == 3) wait_vm<12>(); else if (younger == 2) wait_vm<8>(); else if (younger == 1) wait_vm<4>(); else wait_vm<0>();
    }
    bar();
    if (t == 0) { issue(19); issue(20); } else if (LK + 4 < 32) issue(LK + 4);
    const unsigned ks = ldsb + (unsigned)(LK % NSLOT) * SLOT;
    f32x16 s0 = f32x16{}, s1 = f32x16{};
#pragma unroll
    for (int s = 0; s < 8; ++s) {
      const bf16x8 a0 = lds_b128(ks + (krd ^ (32u * s)));
      const bf16x8 a1 = lds_b128(ks + 8192u + (krd ^ (32u * s)));
      s0 = MFMA(a0, qf[s], s0);
      s1 = MFMA(a1, qf[s], s1);
    }
    float mx = s0[0];
#pragma unroll
    for (int i = 1; i < 16; ++i) mx = fmaxf(mx, s0[i]);
#pragma unroll
    for (int i = 0; i < 16; ++i) mx = fmaxf(mx, s1[i]);
    {
      auto rr = __builtin_amdgcn_permlane32_swap(__float_as_uint(mx), __float_as_uint(mx), false, false);
      mx = fmaxf(__uint_as_float(rr[0]), __uint_as_float(rr[1]));
    }
    const float mn = fmaxf(m, mx);
    const float alpha = __builtin_amdgcn_exp2f((m - mn) * C);
    m = mn;
    const float nm = -mn * C;
    float ps = 0.f;
#pragma unroll
    for (int i = 0; i < 16; ++i) { s0[i] = __builtin_amdgcn_exp2f(fmaf(s0[i], C, nm)); ps += s0[i]; }
#pragma unroll
    for (int i = 0; i < 16; ++i) { s1[i] = __builtin_amdgcn_exp2f(fmaf(s1[i], C, nm)); ps += s1[i]; }
    l = l * alpha + ps;
    asm volatile("" : "+v"(l));
#pragma unroll
    for (int i = 0; i < 4; ++i) o[i] *= alpha;
    bf16x8 pf[4] = {pack8(s0, 0), pack8(s0, 1), pack8(s1, 0), pack8(s1, 1)};
    {
      const int issued = (t == 0) ? 21 : (LV + 4 > 32 ? 32 : LV + 4);
      const int younger = issued - LV - 1;
      if (younger == 3) wait_vm<12>(); else if (younger == 2) wait_vm<8>(); else if (younger == 1) wait_vm<4>(); else wait_vm<0>();
    }
    bar();
    if (LV + 4 < 32) issue(LV + 4);
    const unsigned vs = ldsb + (unsigned)(LV % NSLOT) * SLOT;
#pragma unroll
    for (int s = 0; s < 4; ++s) {
#pragma unroll
      for (int i = 0; i < 4; ++i) {
        const bf16x8 a = cat(lds_tr(vs + vtb[i] + 4096u * s), lds_tr(vs + vtb[i] + 4096u * s + 2048u));
        o[i] = MFMA(a, pf[s], o[i]);
      }
    }
  }
  {
    auto rr = __builtin_amdgcn_permlane32_swap(__float_as_uint(l), __float_as_uint(l), false, false);
    l = __uint_as_float(rr[0]) + __uint_as_float(rr[1]);
  }
  const float il = 1.0f / l;
  u32x4* dst = ofrag + (((size_t)b * 4 + wid) * NH + h) * 8 * 64 + lane;
#pragma unroll
  for (int i = 0; i < 4; ++i) {
    o[i] *= il;
    dst[(2 * i) * 64] = __builtin_bit_cast(u32x4, pack8(o[i], 0));
    dst[(2 * i + 1) * 64] = __builtin_bit_cast(u32x4, pack8(o[i], 1));
  }
}

constexpr int PF = 8;
__global__ __launch_bounds__(512, 2) void oproj_kernel(const u32x4* __restrict__ ofrag, const __hip_bfloat16* __restrict__ wo,
                                                       __hip_bfloat16* __restrict__ out) {
  extern __shared__ __attribute__((aligned(16))) char lds[];
  const unsigned tid = threadIdx.x, lane = tid & 63u, wid = rfl(tid >> 6), hi = lane >> 5;
  const unsigned bid = blockIdx.x, xcd = bid & 7u, idx = bid >> 3;
  const unsigned mb = xcd * 4u + (idx & 3u), nb = idx >> 2;
  const unsigned ldsb = wid * (PF * 2048u);
  const unsigned wr = lane >> 3, wc = lane & 7u;
  const unsigned wsrc = wr * (DM * 2) + 16u * (wc ^ (((wr >> 1) & 1u) << 2));
  const char* wb = (const char*)wo + (size_t)wid * 512 * DM * 2 + nb * 128u;
  const u32x4* of0 = ofrag + ((size_t)(mb * 2) * NH + wid * 4) * 8 * 64 + lane;
  const unsigned g1 = (lane >> 4) & 1u, q4 = (lane >> 2) & 3u, p4 = lane & 3u;
  const unsigned sp = ((p4 & 1u) << 1) | (p4 >> 1);
  unsigned trb[2];
#pragma unroll
  for (int c = 0; c < 2; ++c) trb[c] = ldsb + (8u * hi + q4) * 128u + 64u * ((unsigned)c ^ (q4 >> 1)) + 32u * g1 + 8u * sp;

  u32x4 bq[PF][2];
  auto issue = [&](int ks) {
    const unsigned dst = ldsb + (unsigned)(ks % PF) * 2048u;
    dma16(wb + (size_t)(ks * 16) * DM * 2, wsrc, dst);
    dma16(wb + (size_t)(ks * 16 + 8) * DM * 2, wsrc, dst + 1024u);
    asm volatile("global_load_dwordx4 %0, %1, off" : "=v"(bq[ks % PF][0]) : "v"(of0 + ks * 64) : "memory");
    asm volatile("global_load_dwordx4 %0, %1, off" : "=v"(bq[ks % PF][1]) : "v"(of0 + NH * 8 * 64 + ks * 64) : "memory");
  };
  f32x16 acc[2][2];
#pragma unroll
  for (int i = 0; i < 2; ++i)
#pragma unroll
    for (int j = 0; j < 2; ++j) acc[i][j] = f32x16{};
#pragma unroll
  for (int ks = 0; ks < PF; ++ks) issue(ks);
#pragma unroll
  for (int ks = 0; ks < 32; ++ks) {
    const int younger = (ks + PF <= 32 ? PF : 32 - ks) - 1;
    switch (younger) {
      case 7: asm volatile("s_waitcnt vmcnt(28)" : "+v"(bq[ks % PF][0]), "+v"(bq[ks % PF][1])::"memory"); break;
      case 6: asm volatile("s_waitcnt vmcnt(24)" : "+v"(bq[ks % PF][0]), "+v"(bq[ks % PF][1])::"memory"); break;
      case 5: asm volatile("s_waitcnt vmcnt(20)" : "+v"(bq[ks % PF][0]), "+v"(bq[ks % PF][1])::"memory"); break;
      case 4: asm volatile("s_waitcnt vmcnt(16)" : "+v"(bq[ks % PF][0]), "+v"(bq[ks % PF][1])::"memory"); break;
      case 3: asm volatile("s_waitcnt vmcnt(12)" : "+v"(bq[ks % PF][0]), "+v"(bq[ks % PF][1])::"memory"); break;
      case 2: asm volatile("s_waitcnt vmcnt(8)" : "+v"(bq[ks % PF][0]), "+v"(bq[ks % PF][1])::"memory"); break;
      case 1: asm volatile("s_waitcnt vmcnt(4)" : "+v"(bq[ks % PF][0]), "+v"(bq[ks % PF][1])::"memory"); break;
      default: asm volatile("s_waitcnt vmcnt(0)" : "+v"(bq[ks % PF][0]), "+v"(bq[ks % PF][1])::"memory"); break;
    }
    const unsigned so = (unsigned)(ks % PF) * 2048u;
    bf16x8 a[2];
#pragma unroll
    for (int c = 0; c < 2; ++c) a[c] = cat(lds_tr(trb[c] + so), lds_tr(trb[c] + so + 512u));
    const bf16x8 b0 = __builtin_bit_cast(bf16x8, bq[ks % PF][0]), b1 = __builtin_bit_cast(bf16x8, bq[ks % PF][1]);
#pragma unroll
    for (int c = 0; c < 2; ++c) {
      acc[c][0] = MFMA(a[c], b0, acc[c][0]);
      acc[c][1] = MFMA(a[c], b1, acc[c][1]);
    }
    if (ks + PF < 32) {
      asm volatile("s_waitcnt lgkmcnt(0)" ::: "memory");
      __builtin_amdgcn_sched_barrier(0);
      issue(ks + PF);
    }
  }
  __syncthreads();
  f32x4* red = (f32x4*)lds;
#pragma unroll
  for (int c = 0; c < 2; ++c)
#pragma unroll
    for (int j = 0; j < 2; ++j)
#pragma unroll
      for (int g = 0; g < 4; ++g) {
        f32x4 v = {acc[c][j][4 * g], acc[c][j][4 * g + 1], acc[c][j][4 * g + 2], acc[c][j][4 * g + 3]};
        red[((wid * 4 + c * 2 + j) * 4 + g) * 64 + lane] = v;
      }
  __syncthreads();
  const unsigned tile = wid >> 1, oct = wid & 1u;
  f32x4 s0 = {0, 0, 0, 0}, s1 = {0, 0, 0, 0};
#pragma unroll
  for (int w = 0; w < 8; ++w) {
    s0 += red[((w * 4 + tile) * 4 + 2 * oct) * 64 + lane];
    s1 += red[((w * 4 + tile) * 4 + 2 * oct + 1) * 64 + lane];
  }
  u32x4 w4 = {cvtpk(s0[0], s0[1]), cvtpk(s0[2], s0[3]), cvtpk(s1[0], s1[1]), cvtpk(s1[2], s1[3])};
  const unsigned c = tile >> 1, j = tile & 1u;
  const unsigned row = mb * 64u + j * 32u + (lane & 31u), col = nb * 64u + c * 32u + oct * 16u + hi * 8u;
  *(u32x4*)((char*)out + ((size_t)row * DM + col) * 2) = w4;
}

extern "C" void kernel_launch(void* const* d_in, const int* in_sizes, int n_in, void* d_out, int out_size, void* d_ws, size_t ws_size,
                              hipStream_t stream) {
  const __hip_bfloat16* x = (const __hip_bfloat16*)d_in[0];
  const __hip_bfloat16* kc = (const __hip_bfloat16*)d_in[1];
  const __hip_bfloat16* vc = (const __hip_bfloat16*)d_in[2];
  const __hip_bfloat16* wq = (const __hip_bfloat16*)d_in[3];
  const __hip_bfloat16* wo = (const __hip_bfloat16*)d_in[4];
  u32x4* ofrag = (u32x4*)d_ws;
  (void)hipFuncSetAttribute((const void*)attn_kernel, hipFuncAttributeMaxDynamicSharedMemorySize, NSLOT * SLOT);
  (void)hipFuncSetAttribute((const void*)oproj_kernel, hipFuncAttributeMaxDynamicSharedMemorySize, 131072);
  attn_kernel<<<512, 256, NSLOT * SLOT, stream>>>(x, kc, vc, wq, ofrag);
  oproj_kernel<<<256, 512, 131072, stream>>>(ofrag, wo, (__hip_bfloat16*)d_out);
}
```

```cpp
#include <hip/hip_runtime.h>
#include <hip/hip_bf16.h>
#include <stdint.h>
#include <utility>

typedef __bf16 bf16x8 __attribute__((ext_vector_type(8)));
typedef __bf16 bf16x4 __attribute__((ext_vector_type(4)));
typedef __bf16 bf16x2 __attribute__((ext_vector_type(2)));
typedef float f32x4 __attribute__((ext_vector_type(4)));
typedef float f32x2 __attribute__((ext_vector_type(2)));
typedef unsigned u32x4 __attribute__((ext_vector_type(4)));
typedef unsigned u32x2 __attribute__((ext_vector_type(2)));
typedef unsigned long long u64;
#define LAS __attribute__((address_space(3)))
#define DI __device__ __forceinline__
#define SB() __builtin_amdgcn_sched_barrier(0)

constexpr int SQ = 128, DM = 512, NH = 32, SK = 512, DH = 128;
constexpr int HD = NH * DH;
constexpr size_t WS_BATCH = 2u << 20;
constexpr size_t WS_O2 = 512u << 10;
constexpr size_t WS_PART = 1u << 20;
constexpr size_t WS_FLAG = 3u << 19;
constexpr size_t WS_PFLAG = WS_FLAG + 128 * 64;
constexpr size_t WS_EPOCH = WS_PFLAG + 64 * 64;
constexpr unsigned L_W = 0, L_H = 32768, L_KV = 65536;
constexpr float CS = 0.08838834764831845f * 1.4426950408889634f;
constexpr float BIAS = 8.0f;

DI unsigned rfl(unsigned x) { return __builtin_amdgcn_readfirstlane(x); }
DI unsigned cvtpk(float lo, float hi) {
  f32x2 v = {lo, hi};
  bf16x2 b = __builtin_convertvector(v, bf16x2);
  return __builtin_bit_cast(unsigned, b);
}
extern "C" __device__ float mul1(float, float) __asm("llvm.amdgcn.fmul.legacy");
DI u32x2 pack4(const f32x4& a) { u32x2 w = {cvtpk(a[0], a[1]), cvtpk(a[2], a[3])}; return w; }
DI u32x2 pack4(const f32x4& a, float f) { u32x2 w = {cvtpk(mul1(a[0], f), mul1(a[1], f)), cvtpk(mul1(a[2], f), mul1(a[3], f))}; return w; }
DI u32x4 cat2(u32x2 a, u32x2 b) { u32x4 w = {a[0], a[1], b[0], b[1]}; return w; }
#define MFMA(a, b, c) __builtin_amdgcn_mfma_f32_16x16x32_bf16((a), (b), (c), 0, 0, 0)
#define BF(x) __builtin_bit_cast(bf16x8, (x))

DI void dma16(const void* sbase, unsigned voff, unsigned dbase, unsigned doff) {
  asm volatile("s_add_u32 m0, %2, %3\n\ts_nop 0\n\tglobal_load_lds_dwordx4 %0, %1" ::"v"(voff), "s"(sbase), "s"(dbase), "i"(doff) : "memory", "scc");
}
DI void dma16nt(const void* sbase, unsigned voff, unsigned dbase, unsigned doff) {
  asm volatile("s_add_u32 m0, %2, %3\n\ts_nop 0\n\tglobal_load_lds_dwordx4 %0, %1 nt" ::"v"(voff), "s"(sbase), "s"(dbase), "i"(doff) : "memory", "scc");
}
template <int N> DI void wait_vm_c() { asm volatile("s_waitcnt vmcnt(%0)" ::"i"(N) : "memory"); }
DI void waitvm(int n) {
  switch (n) {
#define C1(k) case k: wait_vm_c<k>(); break;
#define C4(k) C1(k) C1(k + 1) C1(k + 2) C1(k + 3)
#define C16(k) C4(k) C4(k + 4) C4(k + 8) C4(k + 12)
    C16(0) C16(16) C16(32) C4(48) C4(52) C4(56) C1(60) C1(61) C1(62)
    default: break;
#undef C1
#undef C4
#undef C16
  }
}
DI void waitlgkm0() { asm volatile("s_waitcnt lgkmcnt(0)" ::: "memory"); }
DI void bar() { SB(); asm volatile("s_waitcnt lgkmcnt(0)\n\ts_barrier" ::: "memory"); SB(); }
DI void bar_nowait() { SB(); asm volatile("s_barrier" ::: "memory"); SB(); }
DI bf16x8 lds_b128(unsigned addr) { return *(const LAS bf16x8*)(uintptr_t)addr; }
DI void lds_st128(unsigned addr, u32x4 v) { *(LAS u32x4*)(uintptr_t)addr = v; }
DI void lds_st64(unsigned addr, u32x2 v) { *(LAS u32x2*)(uintptr_t)addr = v; }
DI float xor16(float v) { return __uint_as_float(__builtin_amdgcn_ds_swizzle(__float_as_uint(v), 0x401F)); }
DI float max_groups(float v) {
  v = fmaxf(v, xor16(v));
  auto rr = __builtin_amdgcn_permlane32_swap(__float_as_uint(v), __float_as_uint(v), false, false);
  return fmaxf(__uint_as_float(rr[0]), __uint_as_float(rr[1]));
}

DI bf16x4 lds_tr(unsigned addr) { return __builtin_amdgcn_ds_read_tr16_b64_v4bf16((LAS bf16x4*)(uintptr_t)addr); }
DI bf16x8 cat(bf16x4 a, bf16x4 b) { return __builtin_shufflevector(a, b, 0, 1, 2, 3, 4, 5, 6, 7); }
DI float fexp2(float x) { return __builtin_amdgcn_exp2f(x); }
DI float max3f(float a, float b, float c) { return fmaxf(fmaxf(a, b), c); }
DI unsigned lane_id() { unsigned l; asm volatile("v_mbcnt_lo_u32_b32 %0, -1, 0\n\tv_mbcnt_hi_u32_b32 %0, -1, %0" : "=v"(l)); return l; }

DI void st16_sc1(const void* sbase, unsigned voff, u32x4 v) { asm volatile("global_store_dwordx4 %0, %1, %2 sc1\n\ts_nop 1" ::"v"(voff), "v"(v), "s"(sbase) : "memory"); }
DI void st8_sc1(const void* sbase, unsigned voff, u64 v) { asm volatile("global_store_dwordx2 %0, %1, %2 sc1" ::"v"(voff), "v"(v), "s"(sbase) : "memory"); }
DI void ld16_sc1(u32x4& d, const void* sbase, unsigned voff) { asm volatile("global_load_dwordx4 %0, %1, %2 sc1" : "=v"(d) : "v"(voff), "s"(sbase) : "memory"); }
DI void ld16_plain(u32x4& d, const void* sbase, unsigned voff) { asm volatile("global_load_dwordx4 %0, %1, %2" : "=v"(d) : "v"(voff), "s"(sbase) : "memory"); }
DI void ld8_sc1(u64& d, const void* sbase, unsigned voff) { asm volatile("global_load_dwordx2 %0, %1, %2 sc1" : "=v"(d) : "v"(voff), "s"(sbase) : "memory"); }
DI void pin8(u64& v) { asm volatile("" : "+v"(v)); }
DI u64 ld8_sc1_wait(const void* sbase, unsigned voff) {
  u64 r; asm volatile("global_load_dwordx2 %0, %1, %2 sc1\n\ts_waitcnt vmcnt(0)" : "=&v"(r) : "v"(voff), "s"(sbase) : "memory"); return r;
}
DI void poll_tag(const void* sbase, unsigned voff, u64 tag) {
  const u64 t0 = __builtin_amdgcn_s_memrealtime();
  for (;;) {
    const u64 v = ld8_sc1_wait(sbase, voff);
    if (__all(v == tag)) break;
    if (__builtin_amdgcn_s_memrealtime() - t0 > 20000000ull) break;
    __builtin_amdgcn_s_sleep(1);
  }
}
template <class F, int... Is> DI void static_for_impl(F&& f, std::integer_sequence<int, Is...>) { (f(std::integral_constant<int, Is>{}), ...); }
template <int N, class F> DI void static_for(F&& f) { static_for_impl(f, std::make_integer_sequence<int, N>{}); }

__attribute__((always_inline)) constexpr bool kv_isk(int e) { return (e == 0) || ((e & 1) && e != 31); }
__attribute__((always_inline)) constexpr int kv_tile(int e) { return e == 0 ? 0 : (e == 31 ? 15 : (kv_isk(e) ? (e + 1) / 2 : e / 2 - 1)); }
__attribute__((always_inline)) constexpr unsigned kv_slot(int e) { return L_KV + (kv_isk(e) ? 0u : 49152u) + (unsigned)(kv_tile(e) % 3) * 16384u; }
#define PIN(x) asm volatile("" : "+v"(x))
__attribute__((always_inline)) constexpr unsigned reg_addr(int r, int i) {
  const int last[6] = {25, 27, 28, 29, 30, 31};
  return r == 0 ? L_W + (unsigned)i * 8192u : r == 1 ? L_H + (unsigned)i * 8192u : kv_slot(last[2 * (r - 2) + (i >> 1)]) + (unsigned)(i & 1) * 8192u;
}
__attribute__((always_inline)) constexpr unsigned w_addr(int c) { const int rg[8] = {0, 1, 2, 0, 3, 1, 2, 4}; return reg_addr(rg[c >> 2], c & 3); }
__attribute__((always_inline)) constexpr unsigned wq_addr(int g) { return (!(g & 4) ? L_H : L_W) + (unsigned)(g & 3) * 8192u; }
constexpr unsigned O1A = reg_addr(4, 0), O1B = reg_addr(4, 2);
constexpr int PACE2 = 3;
constexpr int PCHK = 27;
constexpr int FLN = 110;
constexpr int OL0 = 10, OLS = 2;
__attribute__((always_inline)) constexpr int fin_chunk(int n) { return n < 72 ? 16 + n / 6 : n < 96 ? 28 + ((n - 72) & 7) / 2 : 16 + (n - 96) / 2; }
__attribute__((always_inline)) constexpr int fin_block(int n) { return n < 72 ? 2 + n % 6 : n < 96 ? 2 + 2 * ((n - 72) >> 3) + (n & 1) : n & 1; }
constexpr int FLG = 21;
constexpr int oe_chunk(int p) { return (p >= 20 && p <= 26 && (p & 1) == 0) ? (p - 20) / 2 : -1; }
constexpr unsigned CODE_PF_BYTES = 60672u;
constexpr int FPD = 3;
DI unsigned sw_tr(unsigned row, unsigned c16) { return 16u * (c16 ^ (((row & 7u) ^ ((row >> 1) & 4u)) << 1)); }
DI unsigned sw_k(unsigned row, unsigned c16) { return 16u * (c16 ^ (row & 15u)); }


__global__ __launch_bounds__(512, 2) void mha_kernel(const char* __restrict__ x, const char* __restrict__ kc, const char* __restrict__ vc,
                                                     const char* __restrict__ wq, const char* __restrict__ wo, char* __restrict__ out,
                                                     char* ws) {
  const unsigned wid = rfl(threadIdx.x >> 6), w = wid & 3u;
  const unsigned xcd = blockIdx.x & 7u, idx = blockIdx.x >> 3;
  const unsigned b = (xcd & 1u) * 8u + (idx & 7u), xq = xcd >> 1, j = idx >> 3;
  const unsigned h0 = xq * 8u + j * 2u;
  constexpr unsigned XB[4] = {kv_slot(2), kv_slot(3), kv_slot(4), kv_slot(6)};
  const unsigned r0 = 2u * ((idx + j) & 3u);
  asm volatile("" ::"s"(x), "s"(kc), "s"(vc), "s"(wq), "s"(wo), "s"(out), "s"(ws));
  int nvm = 0;
  int posQ[32], posX[8];
#pragma unroll
  for (int i = 0; i < 32; ++i) posQ[i] = 0;
  unsigned xsrc[2], qsrc[2];
  {
    const unsigned lane = threadIdx.x & 63u;
#pragma unroll
    for (unsigned e = 0; e < 2; ++e) {
      const unsigned xr = 8u * e + (lane >> 3), wr = w * 8u + 4u * e + (lane >> 4);
      xsrc[e] = (w * 32u + xr) * (DM * 2) + 16u * ((lane & 7u) ^ ((xr >> 1) & 7u));
      qsrc[e] = wr * (HD * 2) + sw_tr(wr, lane & 15u);
    }
  }
  auto QI = [&](int g) __attribute__((always_inline)) {
    const char* p = wq + (size_t)(h0 + (g >> 4)) * DH * 2 + (size_t)(((((g & 15) >> 1) + r0) & 7u) * 64u + (g & 1) * 32u) * (HD * 2);
    dma16(p, qsrc[0], w * 2048u, wq_addr(g)); dma16(p, qsrc[1], w * 2048u, wq_addr(g) + 1024u);
    nvm += 2; posQ[g] = nvm;
  };
  auto XI = [&](int c) __attribute__((always_inline)) {
    const char* p = x + (size_t)b * SQ * DM * 2 + ((c + r0) & 7u) * 128u;
#pragma unroll
    for (int q = 0; q < 4; ++q) dma16(p + (q >> 1) * 16 * DM * 2, xsrc[q & 1], w * 4096u, XB[c & 3] + q * 1024u);
    nvm += 4; posX[c] = nvm;
  };
  u64 junk = 0, pc0;
  asm volatile("s_getpc_b64 %0" : "=s"(pc0));
  if (wid >= 4) { XI(0); QI(0); QI(1); XI(1); QI(2); }
  else {
    asm volatile("global_load_dwordx2 %0, %1, %2\n\ts_waitcnt vmcnt(0)" : "=&v"(junk) : "v"(min((idx * 4u + w) * 512u + (threadIdx.x & 63u) * 8u, CODE_PF_BYTES - 8u)), "s"(pc0 & ~(u64)255) : "memory");
  }
  nvm = 14; posX[0] = 4; posQ[0] = 6; posQ[1] = 8; posX[1] = 12; posQ[2] = 14;
  ws += b * WS_BATCH;
  asm volatile("" : "+s"(ws));
  SB();
  const char* kb = kc + ((size_t)b * NH + h0) * SK * DH * 2;
  const char* vb = vc + ((size_t)b * NH + h0) * SK * DH * 2;
  int posKV[32];
#pragma unroll
  for (int i = 0; i < 32; ++i) posKV[i] = 0;
  u64 epoch = *(const __attribute__((address_space(4))) u64*)(uintptr_t)(ws + WS_EPOCH);

  auto q_trb = [&](unsigned c, unsigned lane) { return (8u * (lane >> 4) + ((lane >> 2) & 3u)) * 256u + 32u * (c ^ ((lane >> 2) & 3u) ^ ((lane >> 2) & 4u)) + 8u * (lane & 3u); };
  auto x_rd = [&](int buf, unsigned sk, unsigned lane) { return XB[buf] + w * 4096u + (lane & 15u) * 128u + 16u * ((4u * sk + (lane >> 4)) ^ ((lane & 15u) >> 1)); };
  auto put_q = [&](const f32x4 (&acc)[8][2], unsigned base, unsigned lane) __attribute__((always_inline)) {
    float cs = CS; PIN(cs);
    const unsigned hq = base + ((lane & 15u) + 16u * (lane >> 5)) * 16u + 8u * ((lane >> 4) & 1u);
#pragma unroll
    for (int i = 0; i < 8; ++i)
#pragma unroll
      for (int c = 0; c < 2; ++c) lds_st64(hq + ((i >> 1) * 2 + c) * 1024u + (i & 1) * 512u, pack4(acc[i][c], cs));
  };
  auto get_tag = [&]() { asm volatile("" : "+s"(epoch)); return (epoch + 1u) * 0x9E3779B97F4A7C15ull ^ 0x5851F42D4C957F2Dull; };
  const char* flb = ws + WS_FLAG + (xq * 32u + w) * 64u;

  const char* wob = wo + (size_t)(xq * 8u * DH) * (DM * 2) + j * 256u;
  auto wo_src = [&](int c) __attribute__((always_inline)) { return wob + (size_t)((2u * ((j + ((c >> 2) & 3)) & 3u) + (c >> 4)) * DH + (c & 3) * 32u) * (DM * 2); };
  const char* ofb = ws + (w * 16u + xq * 4u) * 8192u;

  const char* pb = ws + WS_PART + j * 16u * 8192u + w * 2048u;

  if (wid < 4) {
    __builtin_amdgcn_s_setprio(2);
    const unsigned lane = lane_id(), li = lane & 15u, lg = lane >> 4, q4 = (lane >> 2) & 3u, p4 = lane & 3u;
    const u64 tag = get_tag();
    bf16x8 qf[8];
    {
      unsigned trb[8], xrd[4][2];
#pragma unroll
      for (int c = 0; c < 8; ++c) trb[c] = q_trb(c, lane);
#pragma unroll
      for (int e = 0; e < 8; ++e) { xrd[e >> 1][e & 1] = x_rd(e >> 1, e & 1, lane); PIN(xrd[e >> 1][e & 1]); }
      f32x4 acc[8][2];
      bf16x8 af[8], xf[2][4];
      bf16x4 ah[8];
      auto rdx = [&](int c, int q) __attribute__((always_inline)) { xf[c & 1][q] = lds_b128(xrd[c & 3][q >> 1] + (q & 1) * 2048u); };
      auto rd0 = [&](int u, int i) __attribute__((always_inline)) { ah[i] = lds_tr(trb[i] + wq_addr(u)); };
      auto rd1 = [&](int u, int i) __attribute__((always_inline)) { af[i] = cat(ah[i], lds_tr(trb[i ^ 4] + (wq_addr(u) + 1024u))); };
      static_for<16>([&](auto c_u) __attribute__((always_inline)) {
        constexpr int u = decltype(c_u)::value;
        if (u > 0) asm volatile("s_waitcnt lgkmcnt(%0)" ::"i"(2 * FPD) : "memory");
        bar_nowait();
        if (u == 0) {
          rdx(0, 0); rdx(0, 1); rdx(0, 2); rdx(0, 3);
#pragma unroll
          for (int i = 0; i < FPD; ++i) { rd0(0, i); rd1(0, i); }
          SB();
        }
#pragma unroll
        for (int i = 0; i < 8; ++i) {
          const bool more = i + FPD < 8 || u < 15;
          const int gn = i + FPD < 8 ? u : u + 1, in = (i + FPD) & 7;
          acc[i][0] = MFMA(af[i], xf[(u >> 1) & 1][2 * (u & 1)], u == 0 ? f32x4{} : acc[i][0]);
          SB();
          if (more) rd0(gn, in);
          SB();
          acc[i][1] = MFMA(af[i], xf[(u >> 1) & 1][2 * (u & 1) + 1], u == 0 ? f32x4{} : acc[i][1]);
          SB();
          if (more) rd1(gn, in);
          if ((u & 1) && u < 15 && i < 2) { rdx((u + 1) >> 1, 2 * i); rdx((u + 1) >> 1, 2 * i + 1); }
          SB();
        }
      });
      float cs = CS; PIN(cs);
      const unsigned qs = XB[3] + w * 4096u, hq = qs + (li + 16u * (lane >> 5)) * 16u + 8u * (lg & 1u);
#pragma unroll
      for (int c = 0; c < 2; ++c) {
#pragma unroll
        for (int i = 0; i < 8; ++i) lds_st64(hq + (i >> 1) * 1024u + (i & 1) * 512u, pack4(acc[i][c], cs));
        asm volatile("" ::: "memory");
#pragma unroll
        for (int i = 0; i < 4; ++i) qf[2 * i + c] = lds_b128(qs + lane * 16u + i * 1024u);
        asm volatile("" ::: "memory");
      }
    }
    unsigned vtb[8], krd[4];
#pragma unroll
    for (int c = 0; c < 8; ++c) {
      vtb[c] = L_KV + 49152u + (4u * lg + q4) * 256u + 32u * ((unsigned)c ^ q4 ^ (((lg ^ (lg >> 1)) & 1u) << 2)) + 8u * p4;
      PIN(vtb[c]);
    }
#pragma unroll
    for (int c = 0; c < 4; ++c) { krd[c] = L_KV + li * 256u + 16u * ((4u * c + lg) ^ li); PIN(krd[c]); }
    unsigned kpos = 0, vpos = 0;
    f32x4 o[8][2], ls[2], sa[2][4][2], negm[2];
    u32x4 pf[2][2][2];
    unsigned pany = 0;
    bf16x8 fr[16];
    const u32x4 ones = {0x3f803f80u, 0x3f803f80u, 0x3f803f80u, 0x3f803f80u};
    auto zero_o = [&]() {
#pragma unroll
      for (int i = 0; i < 8; ++i) { o[i][0] = f32x4{}; o[i][1] = f32x4{}; }
      ls[0] = f32x4{}; ls[1] = f32x4{};
    };
    zero_o();
    negm[0] = f32x4{}; negm[1] = f32x4{};
    auto rdk = [&](int g) { fr[g] = lds_b128(krd[g >> 2] + (unsigned)(g & 3) * 4096u); };
    auto rdv = [&](int g) { const unsigned a = vtb[g & 7] + 8192u * (g >> 3); fr[g] = cat(lds_tr(a), lds_tr(a + 4096u)); };
#define SEL(t, hf, e) sa[(t) & 1][2 * (hf) + ((e) >> 3)][((e) >> 2) & 1][(e) & 3]
#define PW(t, hf, e) pf[(t) & 1][hf][((e) >> 2) & 1][2 * ((e) >> 3) + (((e) >> 1) & 1)]
    float ex[16];
    auto sm_exp = [&](int t, int hf, int e) { ex[e] = fexp2(SEL(t, hf, e)); };
    auto sm_pack = [&](int t, int hf, int e) {
      PW(t, hf, e - 1) = cvtpk(ex[e - 1], ex[e]);
      if ((e & 3) == 3) pany |= PW(t, hf, e - 3) | PW(t, hf, e - 1);
    };
    auto rowmax = [&](int t, int c) {
      float mx = fmaxf(max3f(sa[t & 1][0][c][0], sa[t & 1][0][c][1], sa[t & 1][0][c][2]), sa[t & 1][0][c][3]);
#pragma unroll
      for (int kb = 1; kb < 4; ++kb) mx = max3f(max3f(mx, sa[t & 1][kb][c][0], sa[t & 1][kb][c][1]), sa[t & 1][kb][c][2], sa[t & 1][kb][c][3]);
      return max_groups(mx);
    };
    auto reref = [&](int t) {
#pragma unroll
      for (int c = 0; c < 2; ++c) {
        const float d = rowmax(t, c) + BIAS;
#pragma unroll
        for (int kb = 0; kb < 4; ++kb) sa[t & 1][kb][c] -= d;
        const float nm = negm[c][0] - d;
        negm[c] = f32x4{nm, nm, nm, nm};
      }
    };
    auto check = [&](int t, bool next) {
      asm volatile("" ::"v"(pf[t & 1][0][0]), "v"(pf[t & 1][0][1]), "v"(pf[t & 1][1][0]), "v"(pf[t & 1][1][1]));
      if (__builtin_expect(__any((pany & 0x40004000u) != 0u), 0)) {
#pragma unroll
        for (int c = 0; c < 2; ++c) {
          const float d = fmaxf(rowmax(t, c) + BIAS, 0.f), al = fexp2(-d);
#pragma unroll
          for (int kb = 0; kb < 4; ++kb) { sa[t & 1][kb][c] -= d; if (next) sa[(t + 1) & 1][kb][c] -= d; }
          negm[c] -= d;
#pragma unroll
          for (int i = 0; i < 8; ++i) o[i][c] *= al;
          ls[c] *= al;
        }
#pragma unroll
        for (int e = 0; e < 32; e += 2) PW(t, e >> 4, e & 15) = cvtpk(fexp2(SEL(t, e >> 4, e & 15)), fexp2(SEL(t, e >> 4, (e & 15) + 1)));
      }
      pany = 0;
    };
    auto keep = [&](int t) { const unsigned pa = pany; asm volatile("" ::"v"(pf[t][0][0]), "v"(pf[t][0][1]), "v"(pa)); };
    auto adv = [&](unsigned& a, int d) { a += d; };
    auto keepk = [&]() { asm volatile("" : "+v"(krd[0]), "+v"(krd[1]), "+v"(krd[2]), "+v"(krd[3])); };
    auto keepv = [&]() { asm volatile("" : "+v"(vtb[0]), "+v"(vtb[1]), "+v"(vtb[2]), "+v"(vtb[3]), "+v"(vtb[4]), "+v"(vtb[5]), "+v"(vtb[6]), "+v"(vtb[7])); };
    auto next_head = [&]() {
      const unsigned h2 = L_H + w * 8192u + lane_id() * 16u;
#pragma unroll
      for (int i = 0; i < 8; ++i) qf[i] = lds_b128(h2 + i * 1024u);
      negm[0] = f32x4{}; negm[1] = f32x4{};
    };
    constexpr int PFD = 3;
    auto part = [&](auto isk_, auto t_, auto sm_, auto pre_, auto nxt_, bool newq = false) __attribute__((always_inline)) {
      constexpr bool isk = decltype(isk_)::value, sm = decltype(sm_)::value, pre = decltype(pre_)::value;
      constexpr int t = decltype(t_)::value, nxt = decltype(nxt_)::value, st = t ^ 1, sh = isk ? 1 : 0;
      bar_nowait();
      if (newq) next_head();
      int step;
      if (isk) { step = kpos == 2 ? -32768 : 16384; kpos = kpos == 2 ? 0 : kpos + 1; }
      else { step = vpos == 2 ? -32768 : 16384; vpos = vpos == 2 ? 0 : vpos + 1; }
      if (!pre) {
#pragma unroll
        for (int g = 0; g < PFD; ++g) { if (isk) rdk(g); else rdv(g); }
      }
      SB();
      static_for<16>([&](auto gc) {
        constexpr int g = decltype(gc)::value;
        static_for<2>([&](auto cc) {
          constexpr int c = decltype(cc)::value;
          if (isk) sa[t][g & 3][c] = MFMA(fr[g], qf[2 * (g >> 2) + c], g < 4 ? negm[c] : sa[t][g & 3][c]);
          else o[g & 7][c] = MFMA(fr[g], BF(pf[t][g >> 3][c]), o[g & 7][c]);
          SB();
          if (c == 0 && sm) { sm_exp(st, sh, g); SB(); }
        });
        if (g + PFD < 16) { if (isk) rdk(g + PFD); else rdv(g + PFD); }
        else if (nxt == 1) rdk(g + PFD - 16);
        else if (nxt == 2) rdv(g + PFD - 16);
        if (isk && g >= 1 && ((g + PFD) & 3) == 0 && g + PFD <= 16) adv(krd[((g + PFD) / 4 - 1) & 3], step);
        if (!isk && g + PFD >= 9 && g + PFD < 17) adv(vtb[(g + PFD - 9) & 7], step);
        if (sm && g >= 2 && (g & 1) == 0) sm_pack(st, sh, g - 1);
        if (!isk && (g & 7) >= 6) { constexpr int c = g & 1; SB(); ls[c] = MFMA(BF(ones), BF(pf[t][g >> 3][c]), ls[c]); }
        SB();
      });
      if (sm) sm_pack(st, sh, 15);
      if (sm && !isk) keep(st);
      if (isk) keepk(); else keepv();
    };
    using Y = std::true_type; using N = std::false_type;
    using I0 = std::integral_constant<int, 0>; using I1 = std::integral_constant<int, 1>; using I2 = std::integral_constant<int, 2>;
    auto pack_o = [&](int i, const float (&il)[2]) { return cat2(pack4(o[i & 6][i & 1], il[i & 1]), pack4(o[(i & 6) + 1][i & 1], il[i & 1])); };

    part(Y{}, I0{}, N{}, N{}, I1{});
    reref(0);
#pragma unroll
    for (int e = 0; e < 16; ++e) { sm_exp(0, 0, e); if (e & 1) sm_pack(0, 0, e); }
#pragma unroll 1
    for (unsigned it = 0; it < 7; ++it) {
      part(Y{}, I1{}, Y{}, Y{}, I2{});
      check(0, true);
      part(N{}, I0{}, Y{}, Y{}, I1{});
      part(Y{}, I0{}, Y{}, Y{}, I2{}, it == 3);
      check(1, true);
      if (it == 3) reref(0);
      part(N{}, I1{}, Y{}, Y{}, I1{});
      if (it == 3) {
        const float il[2] = {__builtin_amdgcn_rcpf(ls[0][0]), __builtin_amdgcn_rcpf(ls[1][0])};
        const unsigned h2 = L_H + w * 8192u + lane_id() * 16u;
#pragma unroll
        for (int i = 0; i < 8; ++i) lds_st128(h2 + i * 1024u, pack_o(i, il));
        zero_o();
        waitlgkm0();
      }
    }
    part(Y{}, I1{}, Y{}, Y{}, I0{});
    check(0, true);
    part(N{}, I0{}, Y{}, N{}, I0{});
#pragma unroll
    for (int e = 0; e < 16; ++e) { sm_exp(1, 1, e); if (e & 1) sm_pack(1, 1, e); }
    check(1, false);
    part(N{}, I1{}, N{}, N{}, I0{});
    const unsigned ln = lane_id();
    const char* ost = ofb + WS_O2 + j * 8192u;
    u32x4 opk[8];
    {
      const float il[2] = {__builtin_amdgcn_rcpf(ls[0][0]), __builtin_amdgcn_rcpf(ls[1][0])};
      const unsigned h1 = (w < 2 ? O1A : O1B) + (w & 1u) * 8192u + ln * 16u;
#pragma unroll
      for (int i = 0; i < 8; ++i) { opk[i] = pack_o(i, il); st16_sc1(ost + (i >> 2) * 4096, ln * 16u + (i & 3) * 1024u, opk[i]); SB(); }
      bar_nowait();
#pragma unroll
      for (int i = 0; i < 8; ++i) lds_st128(h1 + i * 1024u, opk[i]);
    }
    unsigned wsrc[2];
#pragma unroll
    for (unsigned e = 0; e < 2; ++e) wsrc[e] = (w * 8u + 4u * e + (ln >> 4)) * (DM * 2) + sw_tr(w * 8u + 4u * e + (ln >> 4), ln & 15u);
    auto WI = [&](int c) __attribute__((always_inline)) {
      dma16(wo_src(c), wsrc[0], w * 2048u, w_addr(c));
      dma16(wo_src(c), wsrc[1], w * 2048u, w_addr(c) + 1024u);
    };
    wait_vm_c<0>();
    if (ln == 0) st8_sc1(flb + 256, j * 512u, tag);
    bar();
    static_for<8>([&](auto cc) { WI(20 + decltype(cc)::value); });
    wait_vm_c<0>();
    return;
  }

  const unsigned lane = lane_id(), li = lane & 15u, lg = lane >> 4, q4 = (lane >> 2) & 3u, p4 = lane & 3u;
  unsigned ksrc[4], vsrc[4], wsrc[2];
#pragma unroll
  for (unsigned q = 0; q < 4; ++q) {
    ksrc[q] = (w * 16u + 4u * q + lg) * 256u + sw_k(4u * q + lg, li);
    vsrc[q] = (w * 16u + 4u * q + lg) * 256u + sw_tr(4u * q + lg, li);
  }
#pragma unroll
  for (unsigned e = 0; e < 2; ++e) wsrc[e] = (w * 8u + 4u * e + lg) * (DM * 2) + sw_tr(w * 8u + 4u * e + lg, li);
  auto KVI = [&](int e) __attribute__((always_inline)) {
#pragma unroll
    for (int q = 0; q < 4; ++q) dma16nt((kv_isk(e) ? kb : vb) + kv_tile(e) * 16384, kv_isk(e) ? ksrc[q] : vsrc[q], w * 4096u, kv_slot(e) + q * 1024u);
    nvm += 4; posKV[e] = nvm;
  };
  int posW[32];
#pragma unroll
  for (int i = 0; i < 32; ++i) posW[i] = 0;
  auto WI = [&](int c) __attribute__((always_inline)) {
    const char* p = wo_src(c);
    dma16(p, wsrc[0], w * 2048u, w_addr(c));
    dma16(p, wsrc[1], w * 2048u, w_addr(c) + 1024u);
    nvm += 2; posW[c] = nvm;
  };
  unsigned trb[8];
#pragma unroll
  for (int c = 0; c < 8; ++c) trb[c] = q_trb(c, lane);
  f32x4 acc[8][2];
  u32x4 bfr[32];
  bf16x8 af[8];
  bf16x4 ah[8];
  const u64 tag = get_tag();
  {
    static_for<16>([&](auto c_u) __attribute__((always_inline)) {
      constexpr int u = decltype(c_u)::value, un = u + 1 < 16 ? u + 1 : u, cx = u == 0 ? 0 : (u + 1) >> 1;
      waitvm(nvm - (posX[un >> 1] > posQ[un] ? posX[un >> 1] : posQ[un]));
      bar();
      if (u == 0) { QI(3); XI(2); QI(4); }
      if (u == 1) { QI(5); XI(3); QI(6); }
      if (u == 2) { QI(7); XI(4); QI(8); }
      if (u == 3) { QI(9); XI(5); QI(10); }
      if (u == 4) { QI(11); XI(6); }
      if (u == 6) XI(7);
      if (u >= 5 && u <= 8) QI(u + 7);
      if (u == 0 || ((u & 1) && u < 15)) {
#pragma unroll
        for (int q = 0; q < 4; ++q) bfr[(4 * cx + q) & 31] = __builtin_bit_cast(u32x4, lds_b128(x_rd(cx & 3, q >> 1, lane) + (q & 1) * 2048u));
      }
      if (u == 5) KVI(0);
      if (u == 8) KVI(1);
      if (u == 10) QI(16);
      if (u == 12) KVI(2);
      if (u == 13) QI(17);
      if (u == 15) { QI(18); KVI(3); QI(19); }
    });
  }
  auto qstep = [&](int k, bool pre, bool nxt) __attribute__((always_inline)) {
    auto r0_ = [&](int kk, int i) __attribute__((always_inline)) { ah[i] = lds_tr(trb[i] + wq_addr(16 + kk)); };
    auto r1_ = [&](int kk, int i) __attribute__((always_inline)) { af[i] = cat(ah[i], lds_tr(trb[i ^ 4] + (wq_addr(16 + kk) + 1024u))); };
    if (!pre) {
#pragma unroll
      for (int i = 0; i < FPD; ++i) { r0_(k, i); r1_(k, i); }
    }
    SB();
#pragma unroll
    for (int i = 0; i < 8; ++i) {
      const bool more = i + FPD < 8 || nxt;
      const int kn = i + FPD < 8 ? k : k + 1, in = (i + FPD) & 7;
      acc[i][0] = MFMA(af[i], BF(bfr[2 * k]), k == 0 ? f32x4{} : acc[i][0]);
      SB();
      if (more) r0_(kn, in);
      SB();
      acc[i][1] = MFMA(af[i], BF(bfr[2 * k + 1]), k == 0 ? f32x4{} : acc[i][1]);
      SB();
      if (more) r1_(kn, in);
      SB();
    }
  };
  unsigned tbase[8] = {0, 0, 0, 0, 0, 0, 0, 0};
  auto rda0 = [&](int c, int i, int n) __attribute__((always_inline)) {
    const unsigned a = w_addr(c);
    if (a < tbase[i] || a + 8192u > tbase[i] + 65536u) {
      const unsigned nb = a >= 98304u ? 98304u : (a >= 32768u ? 32768u : 0u);
      trb[i] += nb - tbase[i]; PIN(trb[i]); tbase[i] = nb;
    }
    ah[n] = lds_tr(trb[i] + (a - tbase[i]));
  };
  auto rda1 = [&](int c, int i, int n) __attribute__((always_inline)) { af[n] = cat(ah[n], lds_tr(trb[i] + (w_addr(c) - tbase[i] + 4096u))); };
  auto substep = [&](int c, bool pre, int nxt, const u32x4& b0, const u32x4& b1, auto&& io) __attribute__((always_inline)) {
    if (!pre) {
#pragma unroll
      for (int i = 0; i < FPD; ++i) { rda0(c, i, i); rda1(c, i, i); }
    }
    SB();
#pragma unroll
    for (int i = 0; i < 8; ++i) {
      const bool more = i + FPD < 8 || nxt >= 0;
      const int cn = i + FPD < 8 ? c : nxt, in = (i + FPD) & 7;
      acc[i][0] = MFMA(af[i], BF(b0), c == 0 ? f32x4{} : acc[i][0]);
      SB();
      if (more) { if (cn == 100) rda0(fin_chunk(in), fin_block(in), in); else rda0(cn, in, in); }
      SB();
      acc[i][1] = MFMA(af[i], BF(b1), c == 0 ? f32x4{} : acc[i][1]);
      SB();
      if (more) { if (cn == 100) rda1(fin_chunk(in), fin_block(in), in); else rda1(cn, in, in); }
      SB();
      io(i);
      SB();
    }
  };
  const unsigned peer_off = ((j + 1u + (lane < 3u ? lane : 0u)) & 3u) * 512u;
  int posO[8] = {0, 0, 0, 0, 0, 0, 0, 0};
  auto OI4 = [&](int half, int ms, int hf, int k) __attribute__((always_inline)) {
    const char* p = ofb + half * WS_O2 + ((j + ms) & 3u) * 8192u + hf * 4096;
#pragma unroll
    for (int i = 0; i < 4; ++i) ld16_sc1(bfr[k * 4 + i], p, lane * 16u + i * 1024u);
    nvm += 4; posO[k] = nvm;
  };
  auto pin2 = [&](int k) __attribute__((always_inline)) { asm volatile("" : "+v"(bfr[k]), "+v"(bfr[k + 1])); };
  auto pin4 = [&](int k) __attribute__((always_inline)) {
#pragma unroll
    for (int i = 0; i < 4; ++i) asm volatile("" : "+v"(bfr[k * 4 + i]));
  };
  int posS = 0, posP = 0;
  u64 pollv;
  bool early_ok = true;
  static_for<32>([&](auto c_p) __attribute__((always_inline)) {
    constexpr int p = decltype(c_p)::value;
    {
      int need = posKV[p];
      if (p < 29 && posKV[(p + 1) & 31] > need) need = posKV[(p + 1) & 31];
      if (p <= 13 && posQ[(18 + p) & 31] > need) need = posQ[(18 + p) & 31];
      if (oe_chunk(p) >= 0 && posW[oe_chunk(p) & 31] > need) need = posW[oe_chunk(p) & 31];
      if (p == FLG && posS > need) need = posS;
      if (p == PCHK && posP > need) need = posP;
      waitvm(nvm - need);
    }
    bar();
    if (p == 17) {
#pragma unroll
      for (int i = 0; i < 8; ++i) bfr[i] = __builtin_bit_cast(u32x4, lds_b128(L_H + w * 8192u + lane * 16u + i * 1024u));
    }
    if (p == 17 || p == 18) {
#pragma unroll
      for (int i = 0; i < 4; ++i) { st16_sc1(ofb + j * 8192u + (p - 17) * 4096, lane * 16u + i * 1024u, bfr[(4 * (p - 17) + i) & 7]); nvm += 1; posS = nvm; }
    }
    if (p == FLG) { if (lane == 0) st8_sc1(flb, j * 512u, tag); nvm += 1; }
    if (p == PCHK - 2) { ld8_sc1(pollv, flb, peer_off); nvm += 1; posP = nvm; }
    if (p == PCHK) { pin8(pollv); early_ok = __all(pollv == tag); }
    if (p == 0) KVI(4);
    if (p >= 1 && p <= 10) QI(21 + p);
    if (p >= 14 && p <= 21) WI(p - 14);
    if (p == 22) WI(12);
    if (p == 23 || p == 25 || p == 27) WI(13 + (p - 23) / 2);
    if (p == 1) KVI(5);
    else if (p == 2) { KVI(6); KVI(7); }
    else if (p >= 3 && p <= 16) KVI(p + 5);
    else if (p >= 19 && p <= 28) KVI(p + 3);
    if (p >= 3 && p <= 10) {
      ld16_plain(bfr[(p - 3) & 7], wo + (size_t)(xq * 8u * DH) * (DM * 2) + (idx * 4u + w) * 8192u + (p - 3) * 1024, lane * 16u);
      nvm += 1;
    }
    if (p == 12 || p == 23) {
      asm volatile("global_load_dwordx2 %0, %1, %2" : "=v"(junk) : "v"(min((idx * 4u + w) * 512u + lane * 8u, CODE_PF_BYTES - 8u)), "s"(pc0 & ~(u64)255) : "memory");
      nvm += 1;
    }
    if (p == 17 || p == 27) pin8(junk);
    if (p == 16) { pin4(0); pin4(1); }
    if (p == 0) { qstep(0, false, true); QI(20); qstep(1, true, true); qstep(2, true, false); QI(21); }
    else if (p <= 13) {
      __builtin_amdgcn_s_setprio(3);
      qstep((p + 2) & 15, false, false);
      __builtin_amdgcn_s_setprio(0);
    }
    if (PACE2 > 0 && p >= 1 && p <= 12) __builtin_amdgcn_s_sleep(PACE2);
    if (p == 14) {
      __builtin_amdgcn_s_setprio(3);
      put_q(acc, L_H + w * 8192u, lane);
      __builtin_amdgcn_s_setprio(0);
#pragma unroll
      for (int c = 0; c < 8; ++c) trb[c] = (4u * lg + q4) * 256u + 32u * ((((unsigned)c + 2u * xq) & 7u) ^ q4 ^ (((lg ^ (lg >> 1)) & 1u) << 2)) + 8u * p4;
    }
    if (oe_chunk(p) >= 0) {
      constexpr int c = oe_chunk(p) & 3;
      __builtin_amdgcn_s_setprio(3);
      substep(c, false, -1, bfr[2 * c], bfr[2 * c + 1], [&](int) {});
      __builtin_amdgcn_s_setprio(0);
    }
    if (p == 26) WI(8);
    if (p == PCHK) OI4(0, 1, 0, 2);
    if (p == 28) WI(9);
    if (p == 29) { OI4(0, 1, 1, 3); WI(10); }
    if (p == 30) { WI(11); OI4(0, 2, 0, 4); OI4(0, 2, 1, 5); }
    if (p == 31) { WI(16); WI(17); WI(18); WI(19); }
  });
  if (__builtin_expect(!early_ok, 0)) {
    poll_tag(flb, peer_off, tag);
    pin4(2); pin4(3); pin4(4); pin4(5);
    const int keep = nvm, k2 = posO[2], k3 = posO[3], k4 = posO[4], k5 = posO[5];
    OI4(0, 1, 0, 2); OI4(0, 1, 1, 3); OI4(0, 2, 0, 4); OI4(0, 2, 1, 5);
    wait_vm_c<0>();
    pin4(2); pin4(3); pin4(4); pin4(5);
    nvm = keep; posO[2] = k2; posO[3] = k3; posO[4] = k4; posO[5] = k5;
  }
  waitvm(nvm - posO[3]);
  pin4(2); pin4(3);
  const unsigned h1 = (w < 2 ? O1A : O1B) + (w & 1u) * 8192u;
  static_for<12>([&](auto c_c) __attribute__((always_inline)) {
    constexpr int c = 4 + decltype(c_c)::value;
    if (c == 8) {
      wait_vm_c<0>(); pin4(4); pin4(5);
      bar_nowait();
    }
    if (c == 12) {
      wait_vm_c<0>(); pin4(6); pin4(7);
      asm volatile("s_waitcnt lgkmcnt(%0)" ::"i"(2 * FPD) : "memory");
      bar_nowait();
#pragma unroll
      for (int i = 0; i < 8; ++i) bfr[i] = __builtin_bit_cast(u32x4, lds_b128(h1 + lane * 16u + i * 1024u));
    }
    substep(c, c > 4, c < 15 ? c + 1 : 100, bfr[2 * c], bfr[2 * c + 1], [&](int i) {
      if (c == 15 && i == 0) ld8_sc1(pollv, flb + 256, peer_off);
      if (c == 8 && (i == 0 || i == 4)) OI4(0, 3, i >> 2, 6 + (i >> 2));
      if (c == 13) {
        dma16(wo_src(28) + (size_t)(w * 32u + 4u * i) * (DM * 2), lg * (DM * 2) + sw_tr(4u * i + lg, li), h1, i * 1024u);
        nvm += 1;
      }
    });
  });
  asm volatile("s_waitcnt vmcnt(0)" : "+v"(pollv)::"memory");
  if (!__all(pollv == tag)) poll_tag(flb + 256, peer_off, tag);
  const char* fb = ws + WS_PFLAG + (j * 16u + w) * 64u;
  const __amdgpu_buffer_rsrc_t prs = __builtin_amdgcn_make_buffer_rsrc((void*)pb, 0, 16 * 8192, 0x00020000);
  {
    int posL[24];
    auto OL = [&](int k) __attribute__((always_inline)) {
      ld16_sc1(bfr[8 + k], ofb + WS_O2 + ((j + 1u + (k >> 3)) & 3u) * 8192u + ((k >> 2) & 1) * 4096, lane * 16u + (k & 3) * 1024u);
      nvm += 1; posL[k] = nvm;
    };
    static_for<OL0>([&](auto kc) { OL(decltype(kc)::value); });
    static_for<128>([&](auto nc) {
      constexpr int n = decltype(nc)::value, c = fin_chunk(n), i = fin_block(n), bi = 2 * (c - 16);
      if (n == 24 - FPD) bar_nowait();
      if (c >= 20 && n < 80 && (n == 0 || fin_chunk(n - 1) != c)) { waitvm(nvm - posL[(bi + 17) % 24]); pin2(bi); }
      acc[i][0] = MFMA(af[n & 7], BF(bfr[bi]), acc[i][0]);
      SB();
      if (n + FPD < 128) rda0(fin_chunk(n + FPD), fin_block(n + FPD), (n + FPD) & 7);
      SB();
      acc[i][1] = MFMA(af[n & 7], BF(bfr[bi + 1]), acc[i][1]);
      SB();
      if (n + FPD < 128) rda1(fin_chunk(n + FPD), fin_block(n + FPD), (n + FPD) & 7);
      if (n % OLS == 0 && OL0 + n / OLS < 24) { SB(); OL((OL0 + n / OLS) % 24); }
      if (n > 80 && n < 99 && ((n & 7) == 1 || (n & 7) == 2)) {
        constexpr int blk = ((n - 72) >> 3) & 3, q = ((n & 7) - 1) & 1;
        const u32x4 v = cat2(pack4(acc[2 * blk][q]), pack4(acc[2 * blk + 1][q]));
        __builtin_amdgcn_raw_buffer_store_b128(v, prs, lane * 16u + q * 1024u, ((((unsigned)blk + xq) & 3u) * 4u + xq) * 8192u, 16);
      }
      if (n == FLN) { wait_vm_c<0>(); if (lane == 0) st8_sc1(fb, xq * 256u, tag); }
      SB();
    });
  }
  poll_tag(fb, ((xq + 1u + (lane < 3u ? lane : 0u)) & 3u) * 256u, tag);
  {
    u32x4 in[6];
#pragma unroll
    for (int i = 0; i < 6; ++i) ld16_sc1(in[i], pb + (size_t)(xq * 4u + ((xq + 3u - (unsigned)(i >> 1)) & 3u)) * 8192u, lane * 16u + (i & 1) * 1024u);
    asm volatile("s_waitcnt vmcnt(0)" : "+v"(in[0]), "+v"(in[1]), "+v"(in[2]), "+v"(in[3]), "+v"(in[4]), "+v"(in[5])::"memory");
#pragma unroll
    for (int i = 0; i < 6; ++i)
#pragma unroll
      for (int e = 0; e < 4; ++e) {
        acc[e >> 1][i & 1][2 * (e & 1)] += __uint_as_float(in[i][e] << 16);
        acc[e >> 1][i & 1][2 * (e & 1) + 1] += __uint_as_float(in[i][e] & 0xffff0000u);
      }
    char* op = out + ((size_t)(b * SQ + w * 32u + li) * DM + j * 128u + xq * 32u + lg * 4u) * 2;
#pragma unroll
    for (int q = 0; q < 4; ++q) *(u32x2*)(op + (q & 1) * 16 * DM * 2 + (q >> 1) * 32) = pack4(acc[q >> 1][q & 1]);
    if (h0 == 0 && w == 0 && lane == 0) st8_sc1(ws + WS_EPOCH, 0u, epoch + 1u);
  }
}

extern "C" void kernel_launch(void* const* d_in, const int* in_sizes, int n_in, void* d_out, int out_size, void* d_ws, size_t ws_size,
                              hipStream_t stream) {
  (void)hipFuncSetAttribute((const void*)mha_kernel, hipFuncAttributeMaxDynamicSharedMemorySize, 163840);
  mha_kernel<<<256, 512, 163840, stream>>>((const char*)d_in[0], (const char*)d_in[1], (const char*)d_in[2], (const char*)d_in[3],
                                           (const char*)d_in[4], (char*)d_out, (char*)d_ws);
}
```
